```python
import torch

def _softmax(x, axis=-1):
    return torch.softmax(x, dim=axis)
def _gelu(x):
    return torch.nn.functional.gelu(x, approximate="tanh")
def _silu(x):
    return torch.nn.functional.silu(x)
def _sigmoid(x):
    return torch.sigmoid(x)
def _layernorm(x, axis=-1, eps=1e-5):
    m = x.mean(dim=axis, keepdim=True)
    v = x.var(dim=axis, keepdim=True, unbiased=False)
    return (x - m) / torch.sqrt(v + eps)
def _scatter_add(tgt, idx, src, axis=0):
    return tgt.clone().index_add_(axis, idx, src)
def _segment_sum(src, seg, n):
    out = torch.zeros((n,) + src.shape[1:], dtype=src.dtype)
    return out.index_add_(0, seg, src)
def _pad(x, pw):
    flat = tuple(v for pair in reversed(pw) for v in pair)
    return torch.nn.functional.pad(x, flat)

kernel_name = 'sequential_preLN__mha_block_cached__swiglu_ffn__decode'

def setup_inputs():

    _w = (lambda a: (lambda t: t.to(torch.bfloat16) if t.is_floating_point() else t)(torch.as_tensor(a)))
    def _A_setup():
        B, Sq, D = 1, 1, 640
        H, Sk, Dh = 32, 16384, 128
        return {
            "x": _w((lambda *s: torch.randn(*s, dtype=torch.bfloat16) * 0.05)(B, Sq, D)),
            "k_cache": _w((lambda *s: torch.randn(*s, dtype=torch.bfloat16) * 3.2)(B, H, Sk, Dh)),
            "v_cache": _w((lambda *s: torch.randn(*s, dtype=torch.bfloat16) * 0.4)(B, H, Sk, Dh)),
            "wq": _w((lambda *s: torch.randn(*s, dtype=torch.bfloat16) * 3.2)(D, H * Dh) * (D ** -0.5)),
            "wo": _w((lambda *s: torch.randn(*s, dtype=torch.bfloat16) * 0.4)(H * Dh, D) * ((H * Dh) ** -0.5)),
        }
    _A_in = _A_setup()

    def _B_setup():
        B, S, D, F = 1, 1, 640, 1728
        return {
            "x": _w((lambda *s: torch.randn(*s, dtype=torch.bfloat16) * 0.05)(B, S, D)),
            "w_gate": _w((lambda *s: torch.randn(*s, dtype=torch.bfloat16) * 0.05)(D, F)),
            "w_up": _w((lambda *s: torch.randn(*s, dtype=torch.bfloat16) * 0.05)(D, F)),
            "w_down": _w((lambda *s: torch.randn(*s, dtype=torch.bfloat16) * 0.05)(F, D)),
        }
    _B_in = _B_setup()

    return {
        "A_x": _A_in["x"],
        "A_k_cache": _A_in["k_cache"],
        "A_v_cache": _A_in["v_cache"],
        "A_wq": _A_in["wq"],
        "A_wo": _A_in["wo"],
        "B_x": _B_in["x"],
        "B_w_gate": _B_in["w_gate"],
        "B_w_up": _B_in["w_up"],
        "B_w_down": _B_in["w_down"],
    }

def reference(A_x, A_k_cache, A_v_cache, A_wq, A_wo, B_x, B_w_gate, B_w_up, B_w_down):
    def _A_ref(x, k_cache, v_cache, wq, wo):
        B, Sq, D = x.shape
        H, Dh = 32, 128
        q = (lambda x, *d: x.permute(*d))((x @ wq).reshape(B, Sq, H, Dh), 0, 2, 1, 3)
        scores = torch.einsum("bhqd,bhkd->bhqk", q, k_cache) * (Dh ** -0.5)
        p = _softmax(scores, axis=-1)
        o = torch.einsum("bhqk,bhkd->bhqd", p, v_cache)
        return (lambda x, *d: x.permute(*d))(o, 0, 2, 1, 3).reshape(B, Sq, H * Dh) @ wo

    def _B_ref(x, w_gate, w_up, w_down):
        g = _silu(x @ w_gate)
        u = x @ w_up
        return (g * u) @ w_down

    x = A_x
    h = x + _A_ref(_layernorm(x, axis=-1), A_k_cache, A_v_cache, A_wq, A_wo)
    h2 = h + _B_ref(_layernorm(h, axis=-1), B_w_gate, B_w_up, B_w_down)
    return h2
```

```cpp
#include <hip/hip_runtime.h>
#include <hip/hip_bf16.h>
#include <stdint.h>

typedef unsigned short u16;
typedef unsigned v4u __attribute__((ext_vector_type(4)));
typedef __bf16 bf16x2 __attribute__((ext_vector_type(2)));

constexpr int D = 640, H = 32, SK = 16384, DH = 128, F = 1728, NS = 8;

__device__ __forceinline__ float bf2f(u16 b) { return __uint_as_float((unsigned)b << 16); }
__device__ __forceinline__ float lo2f(unsigned u) { return __uint_as_float(u << 16); }
__device__ __forceinline__ float hi2f(unsigned u) { return __uint_as_float(u & 0xffff0000u); }
__device__ __forceinline__ u16 f2bf(float f) { unsigned u = __float_as_uint(f); return (u16)((u + 0x7fffu + ((u >> 16) & 1u)) >> 16); }
__device__ __forceinline__ float rbf(float f) { return bf2f(f2bf(f)); }
__device__ __forceinline__ float dot2(unsigned a, unsigned b, float c) {
  return __builtin_amdgcn_fdot2_f32_bf16(__builtin_bit_cast(bf16x2, a), __builtin_bit_cast(bf16x2, b), c, false);
}
__device__ __forceinline__ float wsum(float v) { for (int o = 32; o; o >>= 1) v += __shfl_xor(v, o); return v; }
__device__ __forceinline__ float wmax(float v) { for (int o = 32; o; o >>= 1) v = fmaxf(v, __shfl_xor(v, o)); return v; }

__device__ void ln640(const float* xs, float* xn, float* red) {
  int tid = threadIdx.x;
  float s = 0.f;
  for (int i = tid; i < D; i += 256) s += xs[i];
  s = wsum(s);
  if ((tid & 63) == 0) red[tid >> 6] = s;
  __syncthreads();
  float mean = (red[0] + red[1] + red[2] + red[3]) * (1.f / D);
  __syncthreads();
  float v = 0.f;
  for (int i = tid; i < D; i += 256) { float d = xs[i] - mean; v += d * d; }
  v = wsum(v);
  if ((tid & 63) == 0) red[tid >> 6] = v;
  __syncthreads();
  float var = (red[0] + red[1] + red[2] + red[3]) * (1.f / D);
  float rs = 1.f / sqrtf(var + 1e-5f);
  for (int i = tid; i < D; i += 256) xn[i] = rbf((xs[i] - mean) * rs);
  __syncthreads();
}

__global__ void k_q(const u16* x, const u16* wq, float* q) {
  __shared__ float xs[D], xn[D], red[4], part[4][64];
  int tid = threadIdx.x;
  for (int i = tid; i < D; i += 256) xs[i] = bf2f(x[i]);
  __syncthreads();
  ln640(xs, xn, red);
  int col = blockIdx.x * 64 + (tid & 63), w = tid >> 6;
  float a = 0.f;
  for (int d = w; d < D; d += 4) a += xn[d] * bf2f(wq[(size_t)d * (H * DH) + col]);
  part[w][tid & 63] = a;
  __syncthreads();
  if (tid < 64) q[blockIdx.x * 64 + tid] = rbf(part[0][tid] + part[1][tid] + part[2][tid] + part[3][tid]);
}

__global__ void __launch_bounds__(256) k_attn(const u16* K, const u16* V, const float* q, float* po, float* pml) {
  __shared__ float sc[SK / NS];
  __shared__ float red[4];
  __shared__ float oacc[4][4][128];
  int s = blockIdx.x, h = blockIdx.y, tid = threadIdx.x, lane = tid & 63, w = tid >> 6;
  int r = lane >> 4, c = lane & 15;
  const int NK = SK / NS;
  const v4u* Kp = (const v4u*)(K + ((size_t)h * SK + (size_t)s * NK) * DH);
  const v4u* Vp = (const v4u*)(V + ((size_t)h * SK + (size_t)s * NK) * DH);
  unsigned qp[4];
  for (int j = 0; j < 4; ++j) qp[j] = (unsigned)f2bf(q[h * DH + 8 * c + 2 * j]) | ((unsigned)f2bf(q[h * DH + 8 * c + 2 * j + 1]) << 16);
  float mx = -INFINITY;
  for (int k0 = w * 4; k0 < NK; k0 += 16) {
    v4u kv = __builtin_nontemporal_load(Kp + (size_t)k0 * 16 + lane);
    float d = dot2(kv.x, qp[0], 0.f); d = dot2(kv.y, qp[1], d); d = dot2(kv.z, qp[2], d); d = dot2(kv.w, qp[3], d);
    d += __shfl_xor(d, 1); d += __shfl_xor(d, 2); d += __shfl_xor(d, 4); d += __shfl_xor(d, 8);
    d *= 0.08838834764831845f;
    if (c == 0) sc[k0 + r] = d;
    mx = fmaxf(mx, d);
  }
  mx = wmax(mx);
  if (lane == 0) red[w] = mx;
  __syncthreads();
  float M = fmaxf(fmaxf(red[0], red[1]), fmaxf(red[2], red[3]));
  __syncthreads();
  float l = 0.f;
  for (int i = tid; i < NK; i += 256) { float p = __expf(sc[i] - M); sc[i] = p; l += p; }
  l = wsum(l);
  if (lane == 0) red[w] = l;
  __syncthreads();
  float L = red[0] + red[1] + red[2] + red[3];
  float acc[8] = {0, 0, 0, 0, 0, 0, 0, 0};
  for (int k0 = w * 4; k0 < NK; k0 += 16) {
    v4u vv = __builtin_nontemporal_load(Vp + (size_t)k0 * 16 + lane);
    float p = sc[k0 + r];
    acc[0] += p * lo2f(vv.x); acc[1] += p * hi2f(vv.x); acc[2] += p * lo2f(vv.y); acc[3] += p * hi2f(vv.y);
    acc[4] += p * lo2f(vv.z); acc[5] += p * hi2f(vv.z); acc[6] += p * lo2f(vv.w); acc[7] += p * hi2f(vv.w);
  }
  for (int j = 0; j < 8; ++j) oacc[w][r][8 * c + j] = acc[j];
  __syncthreads();
  if (tid < 128) {
    float o = 0.f;
    for (int ww = 0; ww < 4; ++ww) for (int rr = 0; rr < 4; ++rr) o += oacc[ww][rr][tid];
    po[(h * NS + s) * DH + tid] = o;
  }
  if (tid == 0) { pml[(h * NS + s) * 2] = M; pml[(h * NS + s) * 2 + 1] = L; }
}

__global__ void __launch_bounds__(256) k_wo(const float* po, const float* pml, const u16* wo, const u16* x, float* hbuf) {
  __shared__ float o[H * DH];
  __shared__ float part[16][16];
  int tid = threadIdx.x;
  for (int i = tid; i < H * DH; i += 256) {
    int h = i >> 7, d = i & 127;
    float M = -INFINITY;
    for (int s = 0; s < NS; ++s) M = fmaxf(M, pml[(h * NS + s) * 2]);
    float L = 0.f, a = 0.f;
    for (int s = 0; s < NS; ++s) { float wgt = __expf(pml[(h * NS + s) * 2] - M); L += wgt * pml[(h * NS + s) * 2 + 1]; a += wgt * po[(h * NS + s) * DH + d]; }
    o[i] = rbf(a / L);
  }
  __syncthreads();
  int c = tid & 15, g = tid >> 4;
  int col = blockIdx.x * 16 + c;
  float a = 0.f;
  for (int k = g; k < H * DH; k += 16) a += o[k] * bf2f(wo[(size_t)k * D + col]);
  part[g][c] = a;
  __syncthreads();
  if (tid < 16) { float t = 0.f; for (int i = 0; i < 16; ++i) t += part[i][tid]; hbuf[blockIdx.x * 16 + tid] = rbf(bf2f(x[blockIdx.x * 16 + tid]) + rbf(t)); }
}

__global__ void __launch_bounds__(256) k_gu(const float* hbuf, const u16* wg, const u16* wu, float* act) {
  __shared__ float xs[D], xn[D], red[4], pg[16][16], pu[16][16];
  int tid = threadIdx.x;
  for (int i = tid; i < D; i += 256) xs[i] = hbuf[i];
  __syncthreads();
  ln640(xs, xn, red);
  int c = tid & 15, g = tid >> 4;
  int col = blockIdx.x * 16 + c;
  float a = 0.f, b = 0.f;
  for (int k = g; k < D; k += 16) { a += xn[k] * bf2f(wg[(size_t)k * F + col]); b += xn[k] * bf2f(wu[(size_t)k * F + col]); }
  pg[g][c] = a; pu[g][c] = b;
  __syncthreads();
  if (tid < 16) {
    float ga = 0.f, ua = 0.f;
    for (int i = 0; i < 16; ++i) { ga += pg[i][tid]; ua += pu[i][tid]; }
    ga = rbf(ga); ua = rbf(ua);
    float sg = rbf(ga / (1.f + __expf(-ga)));
    act[blockIdx.x * 16 + tid] = rbf(sg * ua);
  }
}

__global__ void __launch_bounds__(256) k_down(const float* act, const u16* wd, const float* hbuf, u16* out) {
  __shared__ float a_s[F], part[16][16];
  int tid = threadIdx.x;
  for (int i = tid; i < F; i += 256) a_s[i] = act[i];
  __syncthreads();
  int c = tid & 15, g = tid >> 4;
  int col = blockIdx.x * 16 + c;
  float a = 0.f;
  for (int k = g; k < F; k += 16) a += a_s[k] * bf2f(wd[(size_t)k * D + col]);
  part[g][c] = a;
  __syncthreads();
  if (tid < 16) { float t = 0.f; for (int i = 0; i < 16; ++i) t += part[i][tid]; out[blockIdx.x * 16 + tid] = f2bf(hbuf[blockIdx.x * 16 + tid] + rbf(t)); }
}

extern "C" void kernel_launch(void* const* d_in, const int* in_sizes, int n_in, void* d_out, int out_size, void* d_ws, size_t ws_size, hipStream_t stream) {
  const u16* x = (const u16*)d_in[0];
  const u16* K = (const u16*)d_in[1];
  const u16* V = (const u16*)d_in[2];
  const u16* wq = (const u16*)d_in[3];
  const u16* wo = (const u16*)d_in[4];
  const u16* wg = (const u16*)d_in[6];
  const u16* wu = (const u16*)d_in[7];
  const u16* wd = (const u16*)d_in[8];
  float* ws = (float*)d_ws;
  float* q = ws;
  float* po = ws + 4096;
  float* pml = po + 256 * 128;
  float* hbuf = pml + 512;
  float* act = hbuf + 640;
  k_q<<<64, 256, 0, stream>>>(x, wq, q);
  k_attn<<<dim3(NS, H), 256, 0, stream>>>(K, V, q, po, pml);
  k_wo<<<40, 256, 0, stream>>>(po, pml, wo, x, hbuf);
  k_gu<<<108, 256, 0, stream>>>(hbuf, wg, wu, act);
  k_down<<<40, 256, 0, stream>>>(act, wd, hbuf, (u16*)d_out);
}
```

```cpp
#include <hip/hip_runtime.h>
#include <stdint.h>

typedef unsigned short u16;
typedef unsigned v4u __attribute__((ext_vector_type(4)));
typedef unsigned v2u __attribute__((ext_vector_type(2)));
typedef float f32x4 __attribute__((ext_vector_type(4)));
typedef float f32x2 __attribute__((ext_vector_type(2)));
typedef __bf16 bf16x8 __attribute__((ext_vector_type(8)));
typedef __bf16 bf16x2 __attribute__((ext_vector_type(2)));
typedef short s16x4 __attribute__((ext_vector_type(4)));
#define LAS __attribute__((address_space(3)))
#define GAS __attribute__((address_space(1)))
typedef __amdgpu_buffer_rsrc_t rsrc_t;

#define PACE 1
#define PACE_T0 (30*16)
#define T_TOTAL (3600*16)
#define WQ_AUX 2
#define W_AUX 2
#define LA 12
#define DMAX 4
#define QMODE 0
#define FCAP 15
#define NPRE 6
#define RH 8
#define RC 4

constexpr int D = 640, H = 32, SK = 16384, DH = 128, F = 1728;
constexpr int NW = 8, NT = NW * 64, NWG = 256;
constexpr int NGU = 54, NDN = 40;
constexpr int AUX_NT = 2, AUX_SC1 = 16;
constexpr int RSRC_W3 = 0x00020000;
constexpr unsigned OOB = 0x7fff0000u;
constexpr unsigned SPIN_CAP = 1u << 15;

constexpr unsigned WS_E = 0, WS_TMO = 256, WS_ML = 4096, WS_QA = WS_ML + H * 1024, WS_PA = WS_QA + H * 8 * 1024, WS_YB = WS_PA + H * 8 * 1024,
                   WS_HB = WS_YB + 8 * H * 640, WS_AC = WS_HB + RH * 4096, WS_END = WS_AC + RC * NGU * 128;
constexpr unsigned WS_ST = 1u << 20;
constexpr unsigned L_RED0 = 0, L_RED1 = 4096, L_RED2 = 8192;
constexpr unsigned L_Q = 12288;
constexpr unsigned L_CS = 12544;
constexpr unsigned L_IMG = 12800;
constexpr unsigned L_ST = 16384;
constexpr unsigned L_W = 17408;
constexpr unsigned L_BYTES = L_W + NW * 12288;

__device__ __forceinline__ float lo2f(unsigned u) { return __uint_as_float(u << 16); }
__device__ __forceinline__ float hi2f(unsigned u) { return __uint_as_float(u & 0xffff0000u); }
__device__ __forceinline__ unsigned pk2(float a, float b) { bf16x2 v = {(__bf16)a, (__bf16)b}; return __builtin_bit_cast(unsigned, v); }
__device__ __forceinline__ float rbf(float a) { return lo2f(pk2(a, a)); }
__device__ __forceinline__ float dot2(unsigned a, unsigned b, float c) {
  return __builtin_amdgcn_fdot2_f32_bf16(__builtin_bit_cast(bf16x2, a), __builtin_bit_cast(bf16x2, b), c, false);
}
__device__ __forceinline__ f32x4 mfma(v4u a, v4u b, f32x4 c) {
  return __builtin_amdgcn_mfma_f32_16x16x32_bf16(__builtin_bit_cast(bf16x8, a), __builtin_bit_cast(bf16x8, b), c, 0, 0, 0);
}
template <int CTRL> __device__ __forceinline__ float dpp(float x) {
  return __builtin_bit_cast(float, __builtin_amdgcn_mov_dpp(__builtin_bit_cast(int, x), CTRL, 0xf, 0xf, true));
}
template <int T> __device__ __forceinline__ unsigned bcast(unsigned v) { return __builtin_amdgcn_update_dpp(0u, v, 0x150 + T, 0xf, 0xf, true); }
constexpr int XOR1 = 0xB1, XOR2 = 0x4E, XOR7 = 0x141, XOR8 = 0x128, ROR4 = 0x124, ROR2 = 0x122, ROR1 = 0x121;
__device__ __forceinline__ float x16_32_max(float x) {
  auto s = __builtin_amdgcn_permlane16_swap(__float_as_uint(x), __float_as_uint(x), false, false);
  x = fmaxf(__uint_as_float(s[0]), __uint_as_float(s[1]));
  auto t = __builtin_amdgcn_permlane32_swap(__float_as_uint(x), __float_as_uint(x), false, false);
  return fmaxf(__uint_as_float(t[0]), __uint_as_float(t[1]));
}
__device__ __forceinline__ float x16_32_sum(float x) {
  auto s = __builtin_amdgcn_permlane16_swap(__float_as_uint(x), __float_as_uint(x), false, false);
  x = __uint_as_float(s[0]) + __uint_as_float(s[1]);
  auto t = __builtin_amdgcn_permlane32_swap(__float_as_uint(x), __float_as_uint(x), false, false);
  return __uint_as_float(t[0]) + __uint_as_float(t[1]);
}
__device__ __forceinline__ float wave_max(float x) {
  x = fmaxf(x, dpp<XOR8>(x)); x = fmaxf(x, dpp<ROR4>(x)); x = fmaxf(x, dpp<ROR2>(x)); x = fmaxf(x, dpp<ROR1>(x));
  return x16_32_max(x);
}
__device__ __forceinline__ float wave_sum(float x) {
  x += dpp<XOR8>(x); x += dpp<XOR7>(x); x += dpp<XOR2>(x); x += dpp<XOR1>(x);
  return x16_32_sum(x);
}
template <int AUX> __device__ __forceinline__ v4u ld16(rsrc_t r, unsigned voff, unsigned soff) { return __builtin_amdgcn_raw_buffer_load_b128(r, voff, soff, AUX); }
__device__ __forceinline__ void st8(rsrc_t r, unsigned voff, unsigned val, unsigned tag) { v2u g = {val, tag}; __builtin_amdgcn_raw_buffer_store_b64(g, r, voff, 0, AUX_SC1); }
__device__ __forceinline__ void lds_barrier() { asm volatile("s_waitcnt lgkmcnt(0)\n\ts_barrier" ::: "memory"); }
__device__ __forceinline__ rsrc_t mk(const void* p, unsigned bytes) { return __builtin_amdgcn_make_buffer_rsrc((void*)p, 0, (int)bytes, RSRC_W3); }
__device__ __forceinline__ v4u bmask(unsigned pdup, const v4u& m) { return v4u{pdup & m.x, pdup & m.y, pdup & m.z, pdup & m.w}; }
template <int T> __device__ __forceinline__ v4u bop(unsigned pdup, const v4u& m) {
  return v4u{bcast<T>(pdup) & m.x, bcast<T>(pdup) & m.y, bcast<T>(pdup) & m.z, bcast<T>(pdup) & m.w};
}
template <class Fn> __device__ __forceinline__ void poll(rsrc_t rws, unsigned code, int lane, Fn&& tr) {
#pragma unroll 1
  for (unsigned spins = 0;; ++spins) {
    asm volatile("" ::: "memory");
    if (tr()) break;
    if (spins > SPIN_CAP) { if (lane == 0) __builtin_amdgcn_raw_buffer_store_b32(code, rws, WS_TMO, 0, AUX_SC1); break; }
  }
}
__device__ __forceinline__ bool tags_ok(const v4u& v, unsigned tag) { return v.y == tag && v.w == tag; }
__device__ __forceinline__ unsigned gather8(rsrc_t rws, unsigned base, unsigned tag, unsigned code, int lane, int w) {
  const unsigned off = base + (lane >> 3) * 1024 + (16 * w + 2 * (lane & 7)) * 8;
  v4u v;
  poll(rws, code, lane, [&]() { v = ld16<AUX_SC1>(rws, off, 0); return __all(tags_ok(v, tag)); });
  float a0 = __uint_as_float(v.x), a1 = __uint_as_float(v.z);
  a0 += dpp<XOR8>(a0); a1 += dpp<XOR8>(a1);
  return pk2(x16_32_sum(a0), x16_32_sum(a1));
}
__device__ __forceinline__ float sum8(const LAS float* p) {
  return ((p[0] + p[128]) + (p[256] + p[384])) + ((p[512] + p[640]) + (p[768] + p[896]));
}
__device__ __forceinline__ unsigned fmix(unsigned x) { x += 0x9E3779B9u; x ^= x >> 16; x *= 0x85EBCA6Bu; x ^= x >> 13; x *= 0xC2B2AE35u; x ^= x >> 16; return x; }

#define STAMP(i) do {} while (0)

template <int N> struct IC { static constexpr int v = N; };
template <int N, class Fn> __device__ __forceinline__ void sfor(Fn&& f) {
  if constexpr (N > 0) { sfor<N - 1>(f); f(IC<N - 1>{}); }
}

__global__ void __launch_bounds__(NT, 2) fused_block(const u16* __restrict__ x, const u16* __restrict__ Kc, const u16* __restrict__ Vc, const u16* __restrict__ wq,
                                                     const u16* __restrict__ wo, const u16* __restrict__ wg, const u16* __restrict__ wu, const u16* __restrict__ wd,
                                                     u16* __restrict__ out, unsigned char* ws) {
  extern __shared__ __attribute__((aligned(16))) unsigned char smem[];
  LAS unsigned char* L = (LAS unsigned char*)smem;
  LAS float* red = (LAS float*)(L + L_RED0);
  LAS float* red1 = (LAS float*)(L + L_RED1);
  LAS float* red2 = (LAS float*)(L + L_RED2);
  const unsigned tstart = (unsigned)__builtin_amdgcn_s_memrealtime();
  const int tid = threadIdx.x, lane = tid & 63, w = __builtin_amdgcn_readfirstlane(tid >> 6), b = blockIdx.x;
  const int r = lane >> 4, c = lane & 15;
  const int g8 = b & 7, j32 = b >> 3, h = g8 * 4 + (j32 >> 3), s = j32 & 7;
  const int ugu = (j32 >> 1) * 8 + g8, gdn = g8 + 8 * ((j32 - 8) >> 2);
  const bool is_gu = j32 < 8 && ugu < NGU / 2, is_dn = j32 >= 8 && j32 < 16 && gdn < NDN / 4, is_rd = j32 == 16;
  const int t54 = 2 * ugu + (j32 & 1), td = 4 * gdn + ((j32 - 8) & 3);
  const rsrc_t rws = mk(ws, WS_END), rK = mk(Kc, 128u << 20), rV = mk(Vc, 128u << 20);
  unsigned xs[5];
#pragma unroll
  for (int i = 0; i < 5; ++i) xs[i] = ((const unsigned*)x)[lane + 64 * i];
  float xr[3];
#pragma unroll
  for (int i = 0; i < 3; ++i) xr[i] = lo2f(x[80 * s + 4 * ((w + 8 * i < 20) ? w + 8 * i : 0) + r]);
  v4u wr[3];
  {
    const rsrc_t rq = mk(wq, D * H * DH * 2);
    const unsigned vo = (unsigned)(r * (H * DH * 2) + c * 16), so = (unsigned)((80 * s + 4 * w) * (H * DH * 2) + h * (DH * 2));
    wr[0] = ld16<WQ_AUX>(rq, vo, so); wr[1] = ld16<WQ_AUX>(rq, vo, so + 32 * (H * DH * 2));
    wr[2] = ld16<WQ_AUX>(rq, w < 4 ? vo : OOB, so + 64 * (H * DH * 2));
  }
  const unsigned Ev = *(const GAS unsigned*)(ws + WS_E);
  const unsigned kvo = (unsigned)lane * 16, kvbase = (unsigned)(h * (SK * DH * 2) + s * (SK / 8 * DH * 2) + w * 1024);
  constexpr int RS = 16;
  v4u ring[RS];
  const int nfw = is_gu ? (w < 4 ? 12 : 8) : is_dn ? (w < 6 ? 7 : 6) : 0;
  const unsigned tau = (unsigned)T_TOTAL / (unsigned)(128 + nfw);
  unsigned tnext = (tstart << 4) + PACE_T0;
  auto pace_t = [&]() {
    while ((int)(((unsigned)__builtin_amdgcn_s_memrealtime() << 4) - tnext) < 0) {}
    tnext += tau;
  };
  auto pace = [&]() {
    pace_t();
    if constexpr (DMAX < 16) asm volatile("s_waitcnt vmcnt(%0)" :: "n"(DMAX - 1) : "memory");
  };
  auto kpro = [&](auto N) { if constexpr (N.v < 4) pace_t(); else pace(); ring[N.v] = ld16<AUX_NT>(rK, kvo, kvbase + N.v * 8192); __builtin_amdgcn_sched_barrier(0); };
  __builtin_amdgcn_sched_barrier(0);
  sfor<4>([&](auto N) { kpro(N); });
  const v4u mk4 = {(c >> 1) == 0 ? ((c & 1) ? 0xffff0000u : 0xffffu) : 0u, (c >> 1) == 1 ? ((c & 1) ? 0xffff0000u : 0xffffu) : 0u,
                   (c >> 1) == 2 ? ((c & 1) ? 0xffff0000u : 0xffffu) : 0u, (c >> 1) == 3 ? ((c & 1) ? 0xffff0000u : 0xffffu) : 0u};
  const unsigned E = __builtin_amdgcn_readfirstlane(Ev);
  const unsigned tag = fmix(E);
  v4u q4;
  {
    float sm = 0.f;
#pragma unroll
    for (int i = 0; i < 5; ++i) sm += lo2f(xs[i]) + hi2f(xs[i]);
    const float mean = wave_sum(sm) * (1.f / D);
    float sv = 0.f;
#pragma unroll
    for (int i = 0; i < 5; ++i) { const float a = lo2f(xs[i]) - mean, bb = hi2f(xs[i]) - mean; sv += a * a + bb * bb; }
    const float rs = __builtin_amdgcn_rsqf(wave_sum(sv) * (1.f / D) + 1e-5f);
    f32x4 acc = {0.f, 0.f, 0.f, 0.f};
#pragma unroll
    for (int i = 0; i < 3; ++i) { const float n = (xr[i] - mean) * rs; acc = mfma(wr[i], bmask(pk2(n, n), mk4), acc); }
    STAMP(11);
    if (c < 8) {
#pragma unroll
      for (int k = 0; k < 4; ++k) red[w * 128 + 8 * (4 * r + k) + c] = acc[k];
    }
    lds_barrier();
    if (tid < 128) st8(rws, WS_QA + (h * 8 + s) * 1024 + tid * 8, __float_as_uint(sum8(red + tid)), tag);
    STAMP(12);
    sfor<NPRE - 4>([&](auto N) { kpro(IC<4 + N.v>{}); });
    STAMP(15);
    const unsigned qp = gather8(rws, WS_QA + h * 8192, tag, 0x08u, lane, w);
    if (lane < 8) *(LAS unsigned*)(L + L_Q + (8 * w + lane) * 4) = qp;
    lds_barrier();
    q4 = *(const LAS v4u*)(L + L_Q + c * 16);
    STAMP(1);
    sfor<LA - NPRE>([&](auto N) { kpro(IC<NPRE + N.v>{}); });
  }
  float S0 = 0.f, S1 = 0.f, S2 = 0.f, S3 = 0.f;
#pragma unroll 1
  for (int g = 0; g < 4; ++g) {
    const rsrc_t rN = g == 3 ? rV : rK;
    const unsigned o1 = kvbase + (16 * g + LA) * 8192, o2 = g == 3 ? kvbase : kvbase + (16 * g + 16) * 8192;
    float xv[16];
    sfor<16>([&](auto T) {
      constexpr int t = T.v;
      const v4u kk = ring[t % RS];
      float d = dot2(kk.x, q4.x, 0.f); d = dot2(kk.y, q4.y, d); d = dot2(kk.z, q4.z, d); d = dot2(kk.w, q4.w, d);
      asm volatile("" : "+v"(d));
      xv[t] = d;
      pace();
      if constexpr (t + LA < 16) ring[(t + LA) % RS] = ld16<AUX_NT>(rK, kvo, o1 + t * 8192);
      else ring[(t + LA) % RS] = ld16<AUX_NT>(rN, kvo, o2 + (t + LA - 16) * 8192);
      __builtin_amdgcn_sched_barrier(0);
    });
#define TR_STEP(HALF, CTRL, BIT) _Pragma("unroll") for (int k = 0; k < (HALF); ++k) { \
      const float lo_ = xv[k], hi_ = xv[k + (HALF)]; const float t1_ = lo_ + dpp<CTRL>(lo_), t2_ = hi_ + dpp<CTRL>(hi_); xv[k] = (c & (BIT)) ? t2_ : t1_; }
    TR_STEP(8, XOR8, 8) TR_STEP(4, XOR7, 4) TR_STEP(2, XOR2, 2) TR_STEP(1, XOR1, 1)
#undef TR_STEP
    S0 = S1; S1 = S2; S2 = S3; S3 = xv[0];
  }
  STAMP(2);
  constexpr float C2 = 0.08838834764831845f * 1.4426950408889634f;
  unsigned P0, P1, P2, P3; float mw;
  {
    mw = wave_max(fmaxf(fmaxf(S0, S1), fmaxf(S2, S3))) * C2;
    const float p0 = __builtin_amdgcn_exp2f(fmaf(S0, C2, -mw)), p1 = __builtin_amdgcn_exp2f(fmaf(S1, C2, -mw));
    const float p2 = __builtin_amdgcn_exp2f(fmaf(S2, C2, -mw)), p3 = __builtin_amdgcn_exp2f(fmaf(S3, C2, -mw));
    P0 = pk2(p0, p0); P1 = pk2(p1, p1); P2 = pk2(p2, p2); P3 = pk2(p3, p3);
    const float lw = wave_sum((hi2f(P0) + hi2f(P1)) + (hi2f(P2) + hi2f(P3)));
    if (lane == 0) { v4u gml = {__float_as_uint(mw), tag, __float_as_uint(lw), tag}; __builtin_amdgcn_raw_buffer_store_b128(gml, rws, WS_ML + h * 1024 + (s * 8 + w) * 16, 0, AUX_SC1); }
  }
  v4u fw[12];
  {
    const unsigned pitch = is_gu ? F * 2 : D * 2;
    const unsigned vo = is_gu ? (unsigned)((lane >> 2) * (F * 2) + (lane & 3) * 16) : (unsigned)((lane >> 1) * (D * 2) + (lane & 1) * 16);
    const unsigned so = is_gu ? (unsigned)(32 * w * (F * 2) + t54 * 64) : (unsigned)(32 * w * (D * 2) + td * 32);
    sfor<12>([&](auto I) {
      constexpr int i = I.v;
      const bool on = is_gu ? (i < 8 || w < 4) : (is_dn && i < 7 && w + 8 * i < NGU);
      const void* base = is_gu ? (((i >> 1) & 1) ? (const void*)wu : (const void*)wg) : (const void*)wd;
      const unsigned o = is_gu ? so + ((i / 4) * 256 + (i & 1) * 16) * pitch : so + i * 256 * pitch;
      if (on) pace_t();
      fw[i] = ld16<W_AUX>(mk(base, on ? (unsigned)(D * F * 2) : 0u), vo, o);
      __builtin_amdgcn_sched_barrier(0);
    });
  }
  f32x4 acc = {0.f, 0.f, 0.f, 0.f};
#pragma unroll 1
  for (int g = 0; g < 3; ++g) {
    const unsigned o1 = kvbase + (16 * g + LA) * 8192;
    sfor<16>([&](auto T) {
      constexpr int t = T.v;
      acc = mfma(ring[t % RS], bop<t>(P0, mk4), acc);
      asm volatile("" : "+v"(acc));
      pace_t();
      if (t < 8 && g == 0) asm volatile("s_waitcnt vmcnt(%0)" :: "n"(FCAP) : "memory");
      else asm volatile("s_waitcnt vmcnt(%0)" :: "n"(DMAX - 1) : "memory");
      ring[(t + LA) % RS] = ld16<AUX_NT>(rV, kvo, o1 + t * 8192);
      __builtin_amdgcn_sched_barrier(0);
    });
    P0 = P1; P1 = P2; P2 = P3;
  }
  v4u ml = ld16<AUX_SC1>(rws, WS_ML + h * 1024 + lane * 16, 0);
  v4u wor[4];
  float cw;
  {
    const unsigned o1 = kvbase + (48 + LA) * 8192;
    sfor<16 - LA>([&](auto T) {
      constexpr int t = T.v;
      acc = mfma(ring[t % RS], bop<t>(P0, mk4), acc);
      asm volatile("" : "+v"(acc));
      pace();
      ring[(t + LA) % RS] = ld16<AUX_NT>(rV, kvo, o1 + t * 8192);
      __builtin_amdgcn_sched_barrier(0);
    });
    const rsrc_t rwo = mk(wo, H * DH * D * 2);
    const unsigned vo = c < 10 ? (unsigned)(r * (D * 2) + c * 16) : OOB, so = (unsigned)((h * DH + 16 * w) * (D * 2) + s * 160);
#pragma unroll
    for (int i = 0; i < 4; ++i) wor[i] = ld16<W_AUX>(rwo, vo, so + i * 4 * (D * 2));
    __builtin_amdgcn_sched_barrier(0);
    if (!__all(tags_ok(ml, tag))) poll(rws, 0x10u, lane, [&]() { ml = ld16<AUX_SC1>(rws, WS_ML + h * 1024 + lane * 16, 0); return __all(tags_ok(ml, tag)); });
    const float mi = __uint_as_float(ml.x), li = __uint_as_float(ml.z);
    const float M = wave_max(mi);
    const float Lsum = wave_sum(li * __builtin_amdgcn_exp2f(mi - M));
    cw = __builtin_amdgcn_exp2f(mw - M) * __builtin_amdgcn_rcpf(Lsum);
    asm volatile("" : "+v"(cw));
    __builtin_amdgcn_sched_barrier(0);
    sfor<LA>([&](auto T) {
      constexpr int t = 16 - LA + T.v;
      acc = mfma(ring[t % RS], bop<t>(P0, mk4), acc);
    });
  }
  STAMP(3);
  if (c < 8) {
#pragma unroll
    for (int k = 0; k < 4; ++k) red1[w * 128 + 8 * (4 * r + k) + c] = acc[k] * cw;
  }
  lds_barrier();
  if (tid < 128) st8(rws, WS_PA + (h * 8 + s) * 1024 + tid * 8, __float_as_uint(sum8(red1 + tid)), tag);
  STAMP(4);
  const unsigned opair = gather8(rws, WS_PA + h * 8192, tag, 0x20u, lane, w);
  STAMP(5);
  {
    f32x4 y0 = {0.f, 0.f, 0.f, 0.f}, y1 = {0.f, 0.f, 0.f, 0.f};
    const unsigned sel = (r & 1) ? 0x03020302u : 0x01000100u;
    sfor<4>([&](auto I) {
      constexpr int i = I.v;
      unsigned t = __builtin_amdgcn_update_dpp(0u, opair, 0x150 + 2 * i, 0x3, 0xf, true);
      t = __builtin_amdgcn_update_dpp(t, opair, 0x150 + 2 * i + 1, 0xc, 0xf, true);
      const v4u bb = bmask(__builtin_amdgcn_perm(t, t, sel), mk4);
      if constexpr (i & 1) y1 = mfma(wor[i], bb, y1); else y0 = mfma(wor[i], bb, y0);
    });
    if (c < 8) {
#pragma unroll
      for (int k = 0; k < 4; ++k) if (4 * r + k < 10) red2[w * 128 + 8 * (4 * r + k) + c] = y0[k] + y1[k];
    }
    lds_barrier();
    if (tid < 80) st8(rws, WS_YB + (s * H + h) * 640 + tid * 8, __float_as_uint(sum8(red2 + tid)), tag);
  }
  STAMP(6);
  const int kg = r, q4i = (lane & 15) >> 2, pq = lane & 3;
  if (is_rd) {
    const float xres = tid < 80 ? lo2f(x[80 * g8 + tid]) : 0.f;
    const unsigned off = WS_YB + (g8 * H + 4 * w) * 640 + (lane < 40 ? lane : 0) * 16;
    v4u v0, v1, v2, v3;
    poll(rws, 0x30u, lane, [&]() {
      v0 = ld16<AUX_SC1>(rws, off, 0); v1 = ld16<AUX_SC1>(rws, off + 640, 0); v2 = ld16<AUX_SC1>(rws, off + 1280, 0); v3 = ld16<AUX_SC1>(rws, off + 1920, 0);
      return __all(tags_ok(v0, tag) && tags_ok(v1, tag) && tags_ok(v2, tag) && tags_ok(v3, tag)); });
    if (lane < 40)
      *(LAS f32x2*)(red + w * 128 + 2 * lane) = f32x2{(__uint_as_float(v0.x) + __uint_as_float(v1.x)) + (__uint_as_float(v2.x) + __uint_as_float(v3.x)),
                                                      (__uint_as_float(v0.z) + __uint_as_float(v1.z)) + (__uint_as_float(v2.z) + __uint_as_float(v3.z))};
    lds_barrier();
    if (w < 2) {
      const float hv = xres + rbf(sum8(red + tid));
      const unsigned pr = pk2(hv, dpp<XOR1>(hv));
      if (tid < 80 && !(tid & 1)) {
#pragma unroll
        for (int k = 0; k < RH; ++k) st8(rws, WS_HB + k * 4096 + g8 * 512 + (tid >> 1) * 8, pr, tag);
      }
    }
    STAMP(7);
  } else if (is_gu) {
    LAS unsigned char* stg = L + L_W + w * 12288;
    v4u bw[3][4];
#pragma unroll
    for (int i = 0; i < 12; ++i) *(LAS v4u*)(stg + i * 1024 + lane * 16) = fw[i];
#pragma unroll
    for (int i = 0; i < 3; ++i)
#pragma unroll
      for (int u = 0; u < 4; ++u) {
        const LAS unsigned char* a = stg + (i * 2 + (u >> 1)) * 2048 + (8 * kg + q4i) * 64 + (u & 1) * 32 + pq * 8;
        const v2u lo = __builtin_bit_cast(v2u, __builtin_amdgcn_ds_read_tr16_b64_v4i16((LAS s16x4*)a));
        const v2u hi = __builtin_bit_cast(v2u, __builtin_amdgcn_ds_read_tr16_b64_v4i16((LAS s16x4*)(a + 256)));
        bw[i][u] = v4u{lo.x, lo.y, hi.x, hi.y};
      }
    const v4u ones = {0x3f803f80u, 0x3f803f80u, 0x3f803f80u, 0x3f803f80u};
    {
      f32x4 cs[4];
#pragma unroll
      for (int u = 0; u < 4; ++u) cs[u] = f32x4{0.f, 0.f, 0.f, 0.f};
#pragma unroll
      for (int i = 0; i < 3; ++i)
#pragma unroll
        for (int u = 0; u < 4; ++u) cs[u] = mfma(ones, bw[i][u], cs[u]);
      if (lane < 16) {
#pragma unroll
        for (int u = 0; u < 4; ++u) red1[w * 128 + 16 * u + lane] = cs[u].x;
      }
      lds_barrier();
    }
    const float csum = sum8(red1 + lane);
    STAMP(7);
    {
      const unsigned off = WS_HB + (b % RH) * 4096 + w * 512 + (lane < 20 ? lane : 0) * 16;
      v4u v;
      poll(rws, 0x40u, lane, [&]() { v = ld16<AUX_SC1>(rws, off, 0); return __all(tags_ok(v, tag)); });
      if (lane < 20) *(LAS v2u*)(L + L_IMG + (80 * w + 4 * lane) * 2) = v2u{v.x, v.z};
    }
    STAMP(8);
    lds_barrier();
    f32x4 gu[4], s1 = {0.f, 0.f, 0.f, 0.f}, s2 = {0.f, 0.f, 0.f, 0.f};
#pragma unroll
    for (int u = 0; u < 4; ++u) gu[u] = f32x4{0.f, 0.f, 0.f, 0.f};
#pragma unroll
    for (int i = 0; i < 3; ++i) {
      v4u a = *(const LAS v4u*)(L + L_IMG + (i < 2 || w < 4 ? (w + 8 * i) * 64 : 0) + kg * 16);
      if (i == 2 && w >= 4) a = v4u{0u, 0u, 0u, 0u};
#pragma unroll
      for (int u = 0; u < 4; ++u) gu[u] = mfma(a, bw[i][u], gu[u]);
      s1 = mfma(a, ones, s1); s2 = mfma(a, a, s2);
    }
    if (lane < 16) {
#pragma unroll
      for (int u = 0; u < 4; ++u) red[w * 128 + 16 * u + lane] = gu[u].x;
      if (lane == 0) *(LAS f32x2*)(red + w * 128 + 64) = f32x2{s1.x, s2.x};
    }
    lds_barrier();
    if (w == 0) {
      const float t = sum8(red + lane);
      const float mean = sum8(red + 64) * (1.f / D), ex2 = sum8(red + 65) * (1.f / D);
      const float rs = __builtin_amdgcn_rsqf(fmaxf(ex2 - mean * mean, 0.f) + 1e-5f);
      const float z = rbf((t - mean * csum) * rs);
      auto sw = __builtin_amdgcn_permlane32_swap(__float_as_uint(z), __float_as_uint(z), false, false);
      const float gv = __uint_as_float(sw[0]), uv = __uint_as_float(sw[1]);
      const float sg = rbf(gv * __builtin_amdgcn_rcpf(1.f + __builtin_amdgcn_exp2f(gv * -1.4426950408889634f)));
      const float av = sg * uv;
      const unsigned pr = pk2(av, dpp<XOR1>(av));
      if (lane < 32 && !(lane & 1)) {
#pragma unroll
        for (int k = 0; k < RC; ++k) st8(rws, WS_AC + (k * NGU + t54) * 128 + (lane >> 1) * 8, pr, tag);
      }
    }
    STAMP(9);
  } else if (is_dn) {
    LAS unsigned char* stg = L + L_W + w * 12288;
    v4u bw[7];
#pragma unroll
    for (int i = 0; i < 7; ++i) *(LAS v4u*)(stg + i * 1024 + lane * 16) = fw[i];
#pragma unroll
    for (int i = 0; i < 7; ++i) {
      const LAS unsigned char* a = stg + i * 1024 + (8 * kg + q4i) * 32 + pq * 8;
      const v2u lo = __builtin_bit_cast(v2u, __builtin_amdgcn_ds_read_tr16_b64_v4i16((LAS s16x4*)a));
      const v2u hi = __builtin_bit_cast(v2u, __builtin_amdgcn_ds_read_tr16_b64_v4i16((LAS s16x4*)(a + 128)));
      bw[i] = v4u{lo.x, lo.y, hi.x, hi.y};
    }
    STAMP(7);
    float hres = 0.f;
    if (w == 0) {
      const unsigned off = WS_HB + (b % RH) * 4096 + (td / 5) * 512 + (td % 5) * 64 + (lane & 3) * 16;
      v4u v;
      poll(rws, 0x50u, lane, [&]() { v = ld16<AUX_SC1>(rws, off, 0); return __all(tags_ok(v, tag)); });
      const unsigned e0 = __builtin_amdgcn_ds_bpermute((lane >> 2) * 4, v.x), e1 = __builtin_amdgcn_ds_bpermute((lane >> 2) * 4, v.z);
      const unsigned e = (lane & 2) ? e1 : e0;
      hres = (lane & 1) ? hi2f(e) : lo2f(e);
    }
    lds_barrier();
    STAMP(8);
    if (w < 7) {
      const int line = min(8 * w + (lane >> 3), NGU - 1);
      const unsigned off = WS_AC + (b % RC) * (NGU * 128) + line * 128 + (lane & 7) * 16;
      v4u v;
      poll(rws, 0x60u, lane, [&]() { v = ld16<AUX_SC1>(rws, off, 0); return __all(tags_ok(v, tag)); });
      *(LAS v2u*)(L + L_IMG + line * 64 + (lane & 7) * 8) = v2u{v.x, v.z};
    }
    STAMP(9);
    lds_barrier();
    f32x4 o0 = {0.f, 0.f, 0.f, 0.f}, o1 = {0.f, 0.f, 0.f, 0.f};
#pragma unroll
    for (int i = 0; i < 7; ++i) {
      const v4u a = *(const LAS v4u*)(L + L_IMG + (w + 8 * i < NGU ? w + 8 * i : 0) * 64 + kg * 16);
      if (i & 1) o1 = mfma(a, bw[i], o1); else o0 = mfma(a, bw[i], o0);
    }
    if (lane < 16) red[w * 128 + lane] = o0.x + o1.x;
    lds_barrier();
    if (tid < 16) {
      const float res = hres + rbf(sum8(red + tid));
      out[16 * td + tid] = (u16)pk2(res, res);
    }
    if (b == 64 && tid == 0) __hip_atomic_store((GAS unsigned*)(ws + WS_E), E + 1u, __ATOMIC_RELAXED, __HIP_MEMORY_SCOPE_AGENT);
  }
  STAMP(10);
}

extern "C" void kernel_launch(void* const* d_in, const int* in_sizes, int n_in, void* d_out, int out_size, void* d_ws, size_t ws_size, hipStream_t stream) {
  (void)in_sizes; (void)n_in; (void)out_size; (void)ws_size;
  (void)hipFuncSetAttribute((const void*)fused_block, hipFuncAttributeMaxDynamicSharedMemorySize, (int)L_BYTES);
  hipLaunchKernelGGL(fused_block, dim3(NWG), dim3(NT), L_BYTES, stream, (const u16*)d_in[0], (const u16*)d_in[1], (const u16*)d_in[2], (const u16*)d_in[3],
                     (const u16*)d_in[4], (const u16*)d_in[6], (const u16*)d_in[7], (const u16*)d_in[8], (u16*)d_out, (unsigned char*)d_ws);
}
```

```cpp
#include <hip/hip_runtime.h>
#include <stdint.h>

typedef unsigned short u16;
typedef unsigned v4u __attribute__((ext_vector_type(4)));
typedef unsigned v2u __attribute__((ext_vector_type(2)));
typedef float f32x4 __attribute__((ext_vector_type(4)));
typedef float f32x2 __attribute__((ext_vector_type(2)));
typedef __bf16 bf16x8 __attribute__((ext_vector_type(8)));
typedef __bf16 bf16x2 __attribute__((ext_vector_type(2)));
typedef short s16x4 __attribute__((ext_vector_type(4)));
#define LAS __attribute__((address_space(3)))
#define GAS __attribute__((address_space(1)))
typedef __amdgpu_buffer_rsrc_t rsrc_t;

#define PACE_T0 (30*16)
#define T_TOTAL (3600*16)
#define T_END (3800*16)
#define DMAX 5
#define SLOW_N 6
#define SLOW_X (20*16)
#define QP_AT 22
#define QP2_AT 27
#define WAKE (-40*16)
#define PRE_DT (170*16)
#define RH 2
#define RC 2

constexpr int D = 640, H = 32, SK = 16384, DH = 128, F = 1728;
constexpr int NW = 8, NT = NW * 64, NWG = 256;
constexpr int NS = 6, NG = 6;
constexpr int NGU = 54, NDN = 40, NRD = 20;
constexpr int LA = 32;
constexpr int AUX_NT = 2, AUX_SC1 = 16;
constexpr int RSRC_W3 = 0x00020000;
constexpr unsigned SPIN_CAP = 1u << 15;

constexpr unsigned WS_E = 0, WS_TMO = 256,
                   WS_QB = 4096,
                   WS_R0 = WS_QB + H * 512,
                   WS_RF = WS_R0 + 128,
                   WS_ML = WS_RF + H * 128,
                   WS_PA = WS_ML + H * 1024,
                   WS_YB = WS_PA + H * NS * 1024,
                   WS_HB = WS_YB + 40 * H * 128,
                   WS_ZB = WS_HB + RH * NRD * 128,
                   WS_END = WS_ZB + RC * 2 * NGU * 128;
constexpr unsigned L_RED0 = 0, L_RED1 = 4096;
constexpr unsigned L_CS = 8192;
constexpr unsigned L_O = 12288;
constexpr unsigned L_MISC = 12544;
constexpr unsigned L_IMG = 12800;
constexpr unsigned L_W = 16384;
constexpr unsigned L_BYTES = L_W + 81920;

__device__ __forceinline__ float lo2f(unsigned u) { return __uint_as_float(u << 16); }
__device__ __forceinline__ float hi2f(unsigned u) { return __uint_as_float(u & 0xffff0000u); }
__device__ __forceinline__ unsigned pk2(float a, float b) { bf16x2 v = {(__bf16)a, (__bf16)b}; return __builtin_bit_cast(unsigned, v); }
__device__ __forceinline__ float rbf(float a) { return lo2f(pk2(a, a)); }
__device__ __forceinline__ float dot2(unsigned a, unsigned b, float c) {
  return __builtin_amdgcn_fdot2_f32_bf16(__builtin_bit_cast(bf16x2, a), __builtin_bit_cast(bf16x2, b), c, false);
}
__device__ __forceinline__ f32x4 mfma(v4u a, v4u b, f32x4 c) {
  return __builtin_amdgcn_mfma_f32_16x16x32_bf16(__builtin_bit_cast(bf16x8, a), __builtin_bit_cast(bf16x8, b), c, 0, 0, 0);
}
template <int CTRL> __device__ __forceinline__ float dpp(float x) {
  return __builtin_bit_cast(float, __builtin_amdgcn_mov_dpp(__builtin_bit_cast(int, x), CTRL, 0xf, 0xf, true));
}
template <int T> __device__ __forceinline__ unsigned bcast(unsigned v) { return __builtin_amdgcn_update_dpp(0u, v, 0x150 + T, 0xf, 0xf, true); }
constexpr int XOR1 = 0xB1, XOR2 = 0x4E, XOR7 = 0x141, XOR8 = 0x128, ROR4 = 0x124, ROR2 = 0x122, ROR1 = 0x121;
__device__ __forceinline__ float x16_32_max(float x) {
  auto s = __builtin_amdgcn_permlane16_swap(__float_as_uint(x), __float_as_uint(x), false, false);
  x = fmaxf(__uint_as_float(s[0]), __uint_as_float(s[1]));
  auto t = __builtin_amdgcn_permlane32_swap(__float_as_uint(x), __float_as_uint(x), false, false);
  return fmaxf(__uint_as_float(t[0]), __uint_as_float(t[1]));
}
__device__ __forceinline__ float x16_32_sum(float x) {
  auto s = __builtin_amdgcn_permlane16_swap(__float_as_uint(x), __float_as_uint(x), false, false);
  x = __uint_as_float(s[0]) + __uint_as_float(s[1]);
  auto t = __builtin_amdgcn_permlane32_swap(__float_as_uint(x), __float_as_uint(x), false, false);
  return __uint_as_float(t[0]) + __uint_as_float(t[1]);
}
__device__ __forceinline__ float wave_max(float x) {
  x = fmaxf(x, dpp<XOR8>(x)); x = fmaxf(x, dpp<ROR4>(x)); x = fmaxf(x, dpp<ROR2>(x)); x = fmaxf(x, dpp<ROR1>(x));
  return x16_32_max(x);
}
__device__ __forceinline__ float wave_sum(float x) {
  x += dpp<XOR8>(x); x += dpp<XOR7>(x); x += dpp<XOR2>(x); x += dpp<XOR1>(x);
  return x16_32_sum(x);
}
template <int AUX> __device__ __forceinline__ v4u ld16(rsrc_t r, unsigned voff, unsigned soff) { return __builtin_amdgcn_raw_buffer_load_b128(r, voff, soff, AUX); }
template <int AUX> __device__ __forceinline__ void dma16(rsrc_t r, LAS unsigned char* dst, unsigned voff, unsigned soff) {
  __builtin_amdgcn_raw_ptr_buffer_load_lds(r, (LAS void*)dst, 16, voff, soff, 0, AUX);
}
__device__ __forceinline__ void st8(rsrc_t r, unsigned voff, unsigned val, unsigned tag) { v2u g = {val, tag}; __builtin_amdgcn_raw_buffer_store_b64(g, r, voff, 0, AUX_SC1); }
__device__ __forceinline__ void lds_barrier() { asm volatile("s_waitcnt lgkmcnt(0)\n\ts_barrier" ::: "memory"); }
__device__ __forceinline__ void dma_barrier() { asm volatile("s_waitcnt vmcnt(0) lgkmcnt(0)\n\ts_barrier" ::: "memory"); }
__device__ __forceinline__ rsrc_t mk(const void* p, unsigned bytes) { return __builtin_amdgcn_make_buffer_rsrc((void*)p, 0, (int)bytes, RSRC_W3); }
__device__ __forceinline__ v4u bmask(unsigned pdup, const v4u& m) { return v4u{pdup & m.x, pdup & m.y, pdup & m.z, pdup & m.w}; }
template <int T> __device__ __forceinline__ v4u bop(unsigned pdup, const v4u& m) {
  return v4u{bcast<T>(pdup) & m.x, bcast<T>(pdup) & m.y, bcast<T>(pdup) & m.z, bcast<T>(pdup) & m.w};
}
__device__ __forceinline__ v4u trfrag(const LAS unsigned char* a, unsigned four_rows) {
  const v2u lo = __builtin_bit_cast(v2u, __builtin_amdgcn_ds_read_tr16_b64_v4i16((LAS s16x4*)a));
  const v2u hi = __builtin_bit_cast(v2u, __builtin_amdgcn_ds_read_tr16_b64_v4i16((LAS s16x4*)(a + four_rows)));
  return v4u{lo.x, lo.y, hi.x, hi.y};
}
template <class Fn> __device__ __forceinline__ void poll(rsrc_t rws, unsigned code, int lane, Fn&& tr) {
#pragma unroll 1
  for (unsigned spins = 0;; ++spins) {
    asm volatile("" ::: "memory");
    if (tr()) break;
    if (spins > SPIN_CAP) { if (lane == 0) __builtin_amdgcn_raw_buffer_store_b32(code, rws, WS_TMO, 0, AUX_SC1); break; }
  }
}
__device__ __forceinline__ bool tags_ok(const v4u& v, unsigned tag) { return v.y == tag && v.w == tag; }
__device__ __forceinline__ float sum8(const LAS float* p) {
  return ((p[0] + p[128]) + (p[256] + p[384])) + ((p[512] + p[640]) + (p[768] + p[896]));
}
__device__ __forceinline__ unsigned fmix(unsigned x) { x += 0x9E3779B9u; x ^= x >> 16; x *= 0x85EBCA6Bu; x ^= x >> 13; x *= 0xC2B2AE35u; x ^= x >> 16; return x; }
__device__ __forceinline__ void spin_until(unsigned t16) {
  while ((int)(((unsigned)__builtin_amdgcn_s_memrealtime() << 4) - t16) < 0) {}
}

__device__ __forceinline__ void spin_sleep(unsigned t16) {
  while ((int)(((unsigned)__builtin_amdgcn_s_memrealtime() << 4) - t16) < 0) __builtin_amdgcn_s_sleep(4);
}
__device__ __forceinline__ v4u poll16(rsrc_t rws, unsigned off, unsigned tag, unsigned code, int lane) {
  v4u v;
  poll(rws, code, lane, [&]() { v = ld16<AUX_SC1>(rws, off, 0); return __all(tags_ok(v, tag)); });
  return v;
}
template <bool FAST> __device__ __forceinline__ v2u poll16x4(rsrc_t rws, unsigned off, unsigned tag, unsigned code, int lane) {
  if constexpr (!FAST) {
  const v4u v = poll16(rws, off, tag, code, lane);
  return v2u{v.x, v.z};
  } else {
  unsigned x, z, cnt = SPIN_CAP;
  unsigned long long sc;
#define PL_LOAD(A) "buffer_load_dwordx4 v[" A "], %[off], %[rs], 0 offen sc1\n\t"
#define PL_LOOK(A0, A1, A2, A3, A) \
    "s_waitcnt vmcnt(3)\n\t" \
    "v_cmp_ne_u32 vcc, %[tag], v" A1 "\n\t" "v_cmp_ne_u32 %[sc], %[tag], v" A3 "\n\t" \
    "v_mov_b32 %[x], v" A0 "\n\t" "v_mov_b32 %[z], v" A2 "\n\t" \
    "s_or_b64 vcc, vcc, %[sc]\n\t" "s_cbranch_vccz 9f\n\t" PL_LOAD(A)
  asm volatile(
    "s_nop 4\n\t"
    PL_LOAD("240:243") "s_sleep 5\n\t" PL_LOAD("244:247") "s_sleep 5\n\t" PL_LOAD("248:251") "s_sleep 5\n\t" PL_LOAD("252:255")
    "1:\n\t"
    PL_LOOK("240", "241", "242", "243", "240:243") PL_LOOK("244", "245", "246", "247", "244:247")
    PL_LOOK("248", "249", "250", "251", "248:251") PL_LOOK("252", "253", "254", "255", "252:255")
    "s_sub_u32 %[cnt], %[cnt], 1\n\t" "s_cmp_lg_u32 %[cnt], 0\n\t" "s_cbranch_scc1 1b\n\t"
    "9:"
    : [x] "=&v"(x), [z] "=&v"(z), [sc] "=&s"(sc), [cnt] "+s"(cnt)
    : [off] "v"(off), [rs] "s"(rws), [tag] "s"(tag)
    : "vcc", "scc", "memory", "v240", "v241", "v242", "v243", "v244", "v245", "v246", "v247", "v248", "v249", "v250", "v251", "v252", "v253", "v254", "v255");
#undef PL_LOAD
#undef PL_LOOK
  if (cnt == 0 && lane == 0) __builtin_amdgcn_raw_buffer_store_b32(code, rws, WS_TMO, 0, AUX_SC1);
  return v2u{x, z};
  }
}

template <int N> struct IC { static constexpr int v = N; };
template <int N, class Fn> __device__ __forceinline__ void sfor(Fn&& f) {
  if constexpr (N > 0) { sfor<N - 1>(f); f(IC<N - 1>{}); }
}

__global__ void __launch_bounds__(NT, 2) __attribute__((amdgpu_num_vgpr(240))) fused_block(const u16* __restrict__ x, const u16* __restrict__ Kc, const u16* __restrict__ Vc, const u16* __restrict__ wq,
                                                     const u16* __restrict__ wo, const u16* __restrict__ wg, const u16* __restrict__ wu, const u16* __restrict__ wd,
                                                     u16* __restrict__ out, unsigned char* ws) {
  extern __shared__ __attribute__((aligned(16))) unsigned char smem[];
  LAS unsigned char* L = (LAS unsigned char*)smem;
  LAS float* red = (LAS float*)(L + L_RED0);
  LAS float* red1 = (LAS float*)(L + L_RED1);
  const unsigned tstart = (unsigned)__builtin_amdgcn_s_memrealtime();
  const int tid = threadIdx.x, lane = tid & 63, w = __builtin_amdgcn_readfirstlane(tid >> 6), b = blockIdx.x;
  const int r = lane >> 4, c = lane & 15;
  const int g8 = b & 7, j32 = b >> 3;
  const int kg = r, q4i = c >> 2, pq = lane & 3;
  const rsrc_t rws = mk(ws, WS_END);
  const unsigned Ev = *(const GAS unsigned*)(ws + WS_E);

  unsigned pret = 0;
  if (j32 < 4 * NS) {
    const int h = g8 * 4 + j32 / NS, s = j32 % NS;
    const int u0 = (512 * s) / NS, n = (512 * (s + 1)) / NS - u0;
    const rsrc_t rK = mk(Kc + ((size_t)h * SK + u0 * 32) * DH, n * 8192), rV = mk(Vc + ((size_t)h * SK + u0 * 32) * DH, n * 8192);
    const unsigned kvo = (unsigned)(lane * 16 + w * 1024);
    unsigned tau = (unsigned)(T_TOTAL) / (unsigned)(2 * n);
    v4u ring[LA];
    unsigned tnext = (tstart << 4) + PACE_T0;
    auto pace = [&](bool live) {
      if (live) {
        spin_until(tnext); tnext += tau;
        asm volatile("s_waitcnt vmcnt(%0)" :: "n"(DMAX - 1) : "memory");
      }
    };
    volatile LAS unsigned* mslot = (volatile LAS unsigned*)(L + L_MISC);
    if (tid < NW) mslot[tid] = 0xffffffffu;
    lds_barrier();
    const unsigned qoff = WS_QB + h * 512 + c * 32;
    v4u qa, qb, rf, qc, qd;
    sfor<LA>([&](auto N) {
      if constexpr (N.v <= QP_AT) pace(true);
      else {
        spin_until(tnext); tnext += tau;
        asm volatile("s_waitcnt vmcnt(%0)" :: "n"(DMAX + (N.v > QP2_AT ? 4 : 2)) : "memory");
      }
      ring[N.v] = ld16<AUX_NT>(rK, kvo, N.v * 8192);
      if constexpr (N.v < SLOW_N) tnext += SLOW_X;
      if constexpr (N.v == QP_AT) {
        const unsigned oob = w == 0 ? 0u : 0x7ff00000u;
        qa = ld16<AUX_SC1>(rws, qoff + oob, 0); qb = ld16<AUX_SC1>(rws, qoff + 16 + oob, 0); rf = ld16<AUX_SC1>(rws, WS_RF + h * 128 + oob, 0);
      }
      if constexpr (N.v == QP2_AT) {
        const unsigned oob = w == 0 ? 0u : 0x7ff00000u;
        qc = ld16<AUX_SC1>(rws, qoff + oob, 0); qd = ld16<AUX_SC1>(rws, qoff + 16 + oob, 0);
      }
      __builtin_amdgcn_sched_barrier(0);
    });
    const unsigned tag = fmix(__builtin_amdgcn_readfirstlane(Ev));
    if (w == 0) {
      if (!__all(tags_ok(qa, tag) && tags_ok(qb, tag))) { asm volatile("" ::: "memory"); qa = qc; qb = qd; }
      if (!__all(tags_ok(qa, tag) && tags_ok(qb, tag)))
        poll(rws, 0x08u, lane, [&]() { qa = ld16<AUX_SC1>(rws, qoff, 0); qb = ld16<AUX_SC1>(rws, qoff + 16, 0); return __all(tags_ok(qa, tag) && tags_ok(qb, tag)); });
      if (r == 0) *(LAS v4u*)(L + L_IMG + c * 16) = v4u{qa.x, qa.z, qb.x, qb.z};
      if (lane == 0) *(LAS v2u*)(L + L_MISC + 48) = v2u{rf.x, rf.y};
    }
    lds_barrier();
    const v4u q4 = *(const LAS v4u*)(L + L_IMG + c * 16);
    {
      const v2u rfl = *(const LAS v2u*)(L + L_MISC + 48);
      if (__builtin_amdgcn_readfirstlane(rfl.y) == tag) {
        const unsigned now = (unsigned)__builtin_amdgcn_s_memrealtime() << 4;
        if ((int)(now - tnext) > 0) tnext = now;
        const int left = (int)(((unsigned)__builtin_amdgcn_readfirstlane(rfl.x) << 4) + (unsigned)(T_END) - tnext);
        const int t = left / (2 * n - LA);
        tau = (unsigned)min(max(t, (int)tau - (int)(tau >> 3)), (int)tau + (int)(tau >> 3));
      }
    }
    float S[NG];
#pragma unroll
    for (int g = 0; g < NG; ++g) S[g] = 0.f;
    auto kgroup = [&](auto PAR, auto MODE, int g) {
      constexpr int o = 16 * PAR.v, mode = MODE.v;
      float xv[16];
      sfor<16>([&](auto T) {
        constexpr int t = T.v;
        const v4u kk = ring[o + t];
        float d = dot2(kk.x, q4.x, 0.f); d = dot2(kk.y, q4.y, d); d = dot2(kk.z, q4.z, d); d = dot2(kk.w, q4.w, d);
        asm volatile("" : "+v"(d));
        xv[t] = d;
        if constexpr (mode) { pace(true); ring[o + t] = ld16<AUX_NT>(rV, kvo, (o + t) * 8192); }
        else { const int i = 16 * g + LA + t; pace(i < n); ring[o + t] = ld16<AUX_NT>(rK, kvo, i * 8192); }
        __builtin_amdgcn_sched_barrier(0);
      });
#define TR_STEP(HALF, CTRL, BIT) _Pragma("unroll") for (int k = 0; k < (HALF); ++k) { \
        const float lo_ = xv[k], hi_ = xv[k + (HALF)]; const float t1_ = lo_ + dpp<CTRL>(lo_), t2_ = hi_ + dpp<CTRL>(hi_); xv[k] = (c & (BIT)) ? t2_ : t1_; }
      TR_STEP(8, XOR8, 8) TR_STEP(4, XOR7, 4) TR_STEP(2, XOR2, 2) TR_STEP(1, XOR1, 1)
#undef TR_STEP
#pragma unroll
      for (int k = 0; k + 1 < NG; ++k) S[k] = S[k + 1];
      S[NG - 1] = (mode == 2 && 16 * (NG - 1) + c >= n) ? -INFINITY : rbf(rbf(xv[0]) * 0.08838834764831845f);
    };
#pragma unroll 1
    for (int g = 0; g < 4; g += 2) { kgroup(IC<0>{}, IC<0>{}, g); kgroup(IC<1>{}, IC<0>{}, g + 1); }
    kgroup(IC<0>{}, IC<1>{}, 4);
    kgroup(IC<1>{}, IC<2>{}, 5);
    constexpr float C2 = 1.4426950408889634f;
    unsigned P[NG]; float mw;
    {
      float m = S[0];
#pragma unroll
      for (int g = 1; g < NG; ++g) m = fmaxf(m, S[g]);
      mw = wave_max(m) * C2;
      float l = 0.f;
#pragma unroll
      for (int g = 0; g < NG; ++g) { const float p = __builtin_amdgcn_exp2f(fmaf(S[g], C2, -mw)); P[g] = pk2(p, p); l += hi2f(P[g]); }
      const float lw = wave_sum(l);
      if (lane == 0) {
        mslot[w] = __float_as_uint(mw);
        v4u gml = {__float_as_uint(mw), tag, __float_as_uint(lw), tag};
        __builtin_amdgcn_raw_buffer_store_b128(gml, rws, WS_ML + h * 1024 + (s * 8 + w) * 16, 0, AUX_SC1);
      }
    }
    const v4u mk4 = {(c >> 1) == 0 ? ((c & 1) ? 0xffff0000u : 0xffffu) : 0u, (c >> 1) == 1 ? ((c & 1) ? 0xffff0000u : 0xffffu) : 0u,
                     (c >> 1) == 2 ? ((c & 1) ? 0xffff0000u : 0xffffu) : 0u, (c >> 1) == 3 ? ((c & 1) ? 0xffff0000u : 0xffffu) : 0u};
    f32x4 acc = {0.f, 0.f, 0.f, 0.f};
    auto vgroup = [&](auto PAR, int g) {
      constexpr int o = 16 * PAR.v;
      sfor<16>([&](auto T) {
        constexpr int t = T.v;
        acc = mfma(ring[o + t], bop<t>(P[PAR.v], mk4), acc);
        asm volatile("" : "+v"(acc));
        pace(true); ring[o + t] = ld16<AUX_NT>(rV, kvo, (16 * g + LA + t) * 8192);
        __builtin_amdgcn_sched_barrier(0);
      });
    };
    vgroup(IC<0>{}, 0); vgroup(IC<1>{}, 1);
    float cw;
    {
      unsigned mv = mslot[lane & 7];
#pragma unroll 1
      for (unsigned spins = 0; !__all(mv != 0xffffffffu) && spins < SPIN_CAP; ++spins) mv = mslot[lane & 7];
      float M = __uint_as_float(mv);
      M = fmaxf(M, dpp<XOR1>(M)); M = fmaxf(M, dpp<XOR2>(M)); M = fmaxf(M, dpp<XOR7>(M));
      cw = __builtin_amdgcn_exp2f(mw - M);
      asm volatile("" : "+v"(cw));
      __builtin_amdgcn_sched_barrier(0);
    }
    sfor<22>([&](auto K) {
      constexpr int k = 32 + K.v, lo = k == 32 ? 32 : 32 + ((k - 32) * 48 + 21) / 22, hi = 32 + ((k - 31) * 48 + 21) / 22;
      sfor<hi - lo>([&](auto J) { constexpr int j = lo + J.v; acc = mfma(ring[j % 32], bop<j % 16>(P[j / 16], mk4), acc); });
      asm volatile("" : "+v"(acc));
      pace(k + LA < n); ring[k % 32] = ld16<AUX_NT>(rV, kvo, (k + LA) * 8192);
      __builtin_amdgcn_sched_barrier(0);
    });
    sfor<6>([&](auto J) { constexpr int j = 80 + J.v; acc = mfma(ring[j % 32], bop<j % 16>(P[j / 16], mk4), acc); });
    if (c < 8) {
#pragma unroll
      for (int k = 0; k < 4; ++k) red1[w * 128 + 8 * (4 * r + k) + c] = acc[k] * cw;
    }
    lds_barrier();
    if (tid < 128) st8(rws, WS_PA + (h * NS + s) * 1024 + tid * 8, __float_as_uint(sum8(red1 + tid)), tag);
    if (h * NS + s < NGU * 3 && (h * NS + s) % 3 == 0) {
      const int a = (h * NS + s) / 3, mat = a & 1, ct = a >> 1;
      LAS float* red3 = (LAS float*)(L + L_CS);
      v4u bg[3][4];
      {
        const rsrc_t rg = mk(mat ? wu : wg, D * F * 2);
        const unsigned vo = (unsigned)((lane >> 3) * (F * 2) + (lane & 7) * 16 + ct * 128);
#pragma unroll
        for (int i = 0; i < 3; ++i) {
          if (i < 2 || w < 4) {
            const int p = w + 8 * i;
#pragma unroll
            for (int e = 0; e < 4; ++e) dma16<AUX_NT>(rg, L + L_W + p * 4096 + e * 1024, vo, (p * 32 + e * 8) * (F * 2));
          }
        }
        asm volatile("s_waitcnt vmcnt(0)" ::: "memory");
#pragma unroll
        for (int i = 0; i < 3; ++i)
#pragma unroll
          for (int u = 0; u < 4; ++u) bg[i][u] = trfrag(L + L_W + ((i < 2 || w < 4) ? w + 8 * i : w) * 4096 + (8 * kg + q4i) * 128 + u * 32 + pq * 8, 512);
      }
      float csum;
      {
        const v4u ones = {0x3f803f80u, 0x3f803f80u, 0x3f803f80u, 0x3f803f80u};
        f32x4 cs[4];
#pragma unroll
        for (int u = 0; u < 4; ++u) { cs[u] = mfma(ones, bg[0][u], f32x4{0.f, 0.f, 0.f, 0.f}); cs[u] = mfma(ones, bg[1][u], cs[u]); }
        if (w < 4) {
#pragma unroll
          for (int u = 0; u < 4; ++u) cs[u] = mfma(ones, bg[2][u], cs[u]);
        }
        if (lane < 16) {
#pragma unroll
          for (int u = 0; u < 4; ++u) red[w * 128 + 16 * u + lane] = cs[u].x;
        }
        lds_barrier();
        csum = sum8(red + lane);
      }
      unsigned zoff = (lane & 1) ? 0x7ff00000u : WS_ZB + (mat * NGU + 2 * ct) * 128 + (lane >> 1) * 8;
      asm volatile("" : "+v"(zoff));
      if (w >= 1 && w <= 3) {
        const int e = (w - 1) * 64 + lane, line = min(e >> 3, NRD - 1);
        const unsigned off = WS_HB + ((a % RH) * NRD + line) * 128 + (e & 7) * 16;
        *(LAS v2u*)(L + L_IMG + line * 64 + (e & 7) * 8) = poll16x4<true>(rws, off, tag, 0x40u, lane);
      }
      lds_barrier();
      f32x4 gu[4];
      const v4u a0 = *(const LAS v4u*)(L + L_IMG + w * 64 + kg * 16), a1 = *(const LAS v4u*)(L + L_IMG + (w + 8) * 64 + kg * 16);
#pragma unroll
      for (int u = 0; u < 4; ++u) gu[u] = mfma(a0, bg[0][u], f32x4{0.f, 0.f, 0.f, 0.f});
#pragma unroll
      for (int u = 0; u < 4; ++u) gu[u] = mfma(a1, bg[1][u], gu[u]);
      if (w < 4) {
        const v4u a2 = *(const LAS v4u*)(L + L_IMG + (w + 16) * 64 + kg * 16);
#pragma unroll
        for (int u = 0; u < 4; ++u) gu[u] = mfma(a2, bg[2][u], gu[u]);
      } else if (w == 7) {
        float s1 = 0.f, s2 = 0.f;
#pragma unroll
        for (int i = 0; i < 5; ++i) { const unsigned p = *(const LAS unsigned*)(L + L_IMG + (lane + 64 * i) * 4); s1 = dot2(p, 0x3f803f80u, s1); s2 = dot2(p, p, s2); }
        const float mean = wave_sum(s1) * (1.f / D), ex2 = wave_sum(s2) * (1.f / D);
        const float sd = rbf(__builtin_sqrtf(rbf(rbf(fmaxf(ex2 - mean * mean, 0.f)) + 1e-5f)));
        if (lane == 0) *(LAS f32x2*)(L + L_MISC + 40) = f32x2{rbf(mean), __builtin_amdgcn_rcpf(sd)};
      }
      if (lane < 16) {
#pragma unroll
        for (int u = 0; u < 4; ++u) red3[w * 128 + 16 * u + lane] = gu[u].x;
      }
      lds_barrier();
      if (w == 0) {
        const f32x2 st = *(const LAS f32x2*)(L + L_MISC + 40);
        const float z = (sum8(red3 + lane) - st.x * csum) * st.y;
        const unsigned pr = pk2(z, dpp<XOR1>(z));
#pragma unroll
        for (int k = 0; k < RC; ++k) st8(rws, zoff + k * (2 * NGU * 128), pr, tag);
      }
    }
  } else {
    const int t8 = j32 - 4 * NS, T = g8 * 8 + t8, hw = T >> 1, half = T & 1;
    const bool is_rd = T < NRD, is_dn = T >= 64 - NDN;
    const int td = T - (64 - NDN);
    unsigned tag;
    {
      unsigned xs[5];
#pragma unroll
      for (int i = 0; i < 5; ++i) xs[i] = ((const unsigned*)x)[lane + 64 * i];
      float xr[10];
#pragma unroll
      for (int i = 0; i < 10; ++i) xr[i] = lo2f(x[80 * w + 8 * i + 2 * r + (c >> 3)]);
      __builtin_amdgcn_s_barrier();
      v4u wr[10];
      const rsrc_t rq = mk(wq, D * H * DH * 2);
      const unsigned vo = (unsigned)((lane >> 3) * (H * DH * 2) + (lane & 7) * 16), so = (unsigned)(80 * w * (H * DH * 2) + T * 128);
#pragma unroll
      for (int i = 0; i < 10; ++i) wr[i] = ld16<AUX_NT>(rq, vo, so + i * 8 * (H * DH * 2));
      __builtin_amdgcn_sched_barrier(0);
      float sm = 0.f;
#pragma unroll
      for (int i = 0; i < 5; ++i) sm += lo2f(xs[i]) + hi2f(xs[i]);
      const float mean = wave_sum(sm) * (1.f / D);
      float sv = 0.f;
#pragma unroll
      for (int i = 0; i < 5; ++i) { const float a = lo2f(xs[i]) - mean, bb = hi2f(xs[i]) - mean; sv += a * a + bb * bb; }
      const float mb = rbf(mean), isd = 1.f / rbf(__builtin_sqrtf(rbf(rbf(wave_sum(sv) * (1.f / D)) + 1e-5f)));
      const int j8 = c & 7;
      const v4u mk8 = {(j8 >> 1) == 0 ? ((j8 & 1) ? 0xffff0000u : 0xffffu) : 0u, (j8 >> 1) == 1 ? ((j8 & 1) ? 0xffff0000u : 0xffffu) : 0u,
                       (j8 >> 1) == 2 ? ((j8 & 1) ? 0xffff0000u : 0xffffu) : 0u, (j8 >> 1) == 3 ? ((j8 & 1) ? 0xffff0000u : 0xffffu) : 0u};
      f32x4 a0 = {0.f, 0.f, 0.f, 0.f}, a1 = {0.f, 0.f, 0.f, 0.f};
#pragma unroll
      for (int i = 0; i < 10; ++i) {
        const float nn = rbf(xr[i] - mb) * isd;
        const v4u bb = bmask(pk2(nn, nn), mk8);
        if (i & 1) a1 = mfma(wr[i], bb, a1); else a0 = mfma(wr[i], bb, a0);
      }
      if ((r >> 1) == (c >> 3)) {
#pragma unroll
        for (int k = 0; k < 4; ++k) red[w * 128 + (r >> 1) * 64 + (4 * (r & 1) + k) * 8 + j8] = a0[k] + a1[k];
      }
      tag = fmix(__builtin_amdgcn_readfirstlane(Ev));
      if (T == 0 && tid == 0) st8(rws, WS_R0, tstart, tag);
      lds_barrier();
      if (w == NW - 1) {
        v2u g;
        poll(rws, 0x70u, lane, [&]() { g = __builtin_amdgcn_raw_buffer_load_b64(rws, WS_R0, 0, AUX_SC1); return __all(g.y == tag); });
        if (g.y != tag) g.x = tstart;
        if (lane == 0) {
          *(LAS unsigned*)(L + L_MISC + 16) = g.x;
          if (half == 0) __builtin_amdgcn_raw_buffer_store_b128(v4u{g.x, tag, g.x, tag}, rws, WS_RF + hw * 128, 0, AUX_SC1);
        }
      }
      if (w == 0) {
        const float qv = sum8(red + lane) + sum8(red + 64 + lane);
        const unsigned pr = pk2(qv, dpp<XOR1>(qv));
        if (!(lane & 1)) st8(rws, WS_QB + hw * 512 + half * 256 + (lane >> 1) * 8, pr, tag);
      }
    }
    if (w == NW - 2) {
      const unsigned char* p = lane == 0 ? (const unsigned char*)out : lane < 18 ? (const unsigned char*)wg + (lane - 1) * 131072 : (const unsigned char*)wu + (lane - 18) * 131072;
      if (lane < 35) pret = *(const GAS unsigned*)p;
      asm volatile("" ::: "memory");
    }
    v4u bo[3][4], bd[7];
    unsigned tpre = ((unsigned)__builtin_amdgcn_s_memrealtime() << 4);
    auto slot = [&]() { spin_sleep(tpre); tpre += PRE_DT; };
    {
      const rsrc_t ro = mk(wo, H * DH * D * 2);
#pragma unroll
      for (int i = 0; i < 10; ++i) {
        const unsigned off = (unsigned)((w * 10 + i) * 1024 + lane * 16), row = off / 640u;
        slot();
        dma16<AUX_NT>(ro, L + L_W + (w * 10 + i) * 1024, row * (D * 2) + (off - row * 640u), (unsigned)(hw * DH * (D * 2) + half * 640));
      }
      dma_barrier();
#pragma unroll
      for (int i = 0; i < 3; ++i)
#pragma unroll
        for (int ks = 0; ks < 4; ++ks) bo[i][ks] = trfrag(L + L_W + (32 * ks + 8 * kg + q4i) * 640 + ((i < 2 || w < 4) ? w + 8 * i : 0) * 32 + pq * 8, 4 * 640);
      lds_barrier();
    }
    if (is_dn) {
      const rsrc_t rd = mk(wd, F * D * 2);
      const unsigned vo = (unsigned)((lane >> 1) * (D * 2) + (lane & 1) * 16 + td * 32);
#pragma unroll
      for (int i = 0; i < 7; ++i)
        if (w + 8 * i < NGU) { slot(); dma16<AUX_NT>(rd, L + L_W + (w + 8 * i) * 1024, vo, (w + 8 * i) * 32 * (D * 2)); }
      asm volatile("s_waitcnt vmcnt(0)" ::: "memory");
#pragma unroll
      for (int i = 0; i < 7; ++i) bd[i] = trfrag(L + L_W + (w + 8 * i < NGU ? w + 8 * i : w) * 1024 + (8 * kg + q4i) * 32 + pq * 8, 128);
    } else {
#pragma unroll
      for (int i = 0; i < 7; ++i) bd[i] = v4u{0u, 0u, 0u, 0u};
    }
    const float xres = (is_rd && tid < 64) ? lo2f(x[32 * T + (tid & 31)]) : 0.f;
    {
      const unsigned tref = (__builtin_amdgcn_readfirstlane(*(const LAS unsigned*)(L + L_MISC + 16)) << 4) + (unsigned)(T_END);
      while ((int)(((unsigned)__builtin_amdgcn_s_memrealtime() << 4) - (tref - WAKE)) < 0) __builtin_amdgcn_s_sleep(8);
    }
    float csp;
    {
      const bool live = lane < NS * 8;
      const v4u ml = poll16(rws, WS_ML + hw * 1024 + (live ? lane : 0) * 16, tag, 0x10u, lane);
      const float mi = live ? __uint_as_float(ml.x) : -INFINITY, li = live ? __uint_as_float(ml.z) : 0.f;
      float Ms = mi;
      Ms = fmaxf(Ms, dpp<XOR1>(Ms)); Ms = fmaxf(Ms, dpp<XOR2>(Ms)); Ms = fmaxf(Ms, dpp<XOR7>(Ms));
      const float M = wave_max(mi);
      const float Ls = wave_sum(li * __builtin_amdgcn_exp2f(mi - M));
      csp = __builtin_amdgcn_exp2f(Ms - M) * __builtin_amdgcn_rcpf(Ls);
      asm volatile("" : "+v"(csp));
    }
    unsigned yoff = (r < 2 || (r == 2 && w < 4)) ? WS_YB + ((half * 20 + w + 8 * r) * H + hw) * 128 + c * 8 : 0x7ff00000u;
    asm volatile("" : "+v"(yoff));
    {
      const int sp = lane >> 3;
      const unsigned off = WS_PA + (hw * NS + (sp < NS ? sp : 0)) * 1024 + (16 * w + 2 * (lane & 7)) * 8;
      const v2u v = poll16x4<false>(rws, off, tag, 0x20u, lane);
      float a0 = __uint_as_float(v.x) * csp, a1 = __uint_as_float(v.y) * csp;
      a0 += dpp<XOR8>(a0); a1 += dpp<XOR8>(a1);
      const unsigned op = pk2(x16_32_sum(a0), x16_32_sum(a1));
      if (lane < 8) *(LAS unsigned*)(L + L_O + (8 * w + lane) * 4) = op;
    }
    lds_barrier();
    {
      f32x4 y0 = {0.f, 0.f, 0.f, 0.f}, y1 = y0, y2 = y0;
#pragma unroll
      for (int ks = 0; ks < 4; ++ks) {
        const v4u a = *(const LAS v4u*)(L + L_O + ks * 64 + kg * 16);
        y0 = mfma(a, bo[0][ks], y0); y1 = mfma(a, bo[1][ks], y1);
        if (w < 4) y2 = mfma(a, bo[2][ks], y2);
      }
      st8(rws, yoff, __float_as_uint(r == 0 ? y0.x : r == 1 ? y1.x : y2.x), tag);
    }
    if (is_rd) {
      const unsigned off = WS_YB + ((2 * T + ((lane >> 3) & 1)) * H + 4 * w + r) * 128 + (lane & 7) * 16;
      const v2u v = poll16x4<true>(rws, off, tag, 0x30u, lane);
      const float a0 = x16_32_sum(__uint_as_float(v.x)), a1 = x16_32_sum(__uint_as_float(v.y));
      if (lane < 16) *(LAS f32x2*)(red + w * 128 + 2 * lane) = f32x2{a0, a1};
      lds_barrier();
      if (w == 0) {
        const float hv = xres + rbf(sum8(red + (lane & 31)));
        const unsigned pr = pk2(hv, dpp<XOR1>(hv));
#pragma unroll
        for (int k = 0; k < RH; k += 2)
          st8(rws, (!(lane & 1) && k + (lane >> 5) < RH) ? WS_HB + ((k + (lane >> 5)) * NRD + T) * 128 + ((lane & 31) >> 1) * 8 : 0x7ff00000u, pr, tag);
      }
    }
    if (is_dn) {
      float hres = 0.f;
      if (w == 0) {
        const unsigned off = WS_HB + ((T % RH) * NRD + (td >> 1)) * 128 + (td & 1) * 64 + (lane & 3) * 16;
        const v4u v = poll16(rws, off, tag, 0x50u, lane);
        const unsigned e0 = __builtin_amdgcn_ds_bpermute((lane >> 2) * 4, v.x), e1 = __builtin_amdgcn_ds_bpermute((lane >> 2) * 4, v.z);
        const unsigned e = (lane & 2) ? e1 : e0;
        hres = (lane & 1) ? hi2f(e) : lo2f(e);
      }
      lds_barrier();
      if (w >= 1) {
        const int line = min(8 * (w - 1) + (lane >> 3), NGU - 1);
        const unsigned off = WS_ZB + ((T % RC) * 2 * NGU + line) * 128 + (lane & 7) * 16;
        v4u vg, vu;
        poll(rws, 0x60u, lane, [&]() { vg = ld16<AUX_SC1>(rws, off, 0); vu = ld16<AUX_SC1>(rws, off + NGU * 128, 0); return __all(tags_ok(vg, tag) && tags_ok(vu, tag)); });
        auto act = [](float g, float u) { return rbf(g * __builtin_amdgcn_rcpf(1.f + __builtin_amdgcn_exp2f(g * -1.4426950408889634f))) * u; };
        *(LAS v2u*)(L + L_IMG + line * 64 + (lane & 7) * 8) = v2u{pk2(act(lo2f(vg.x), lo2f(vu.x)), act(hi2f(vg.x), hi2f(vu.x))), pk2(act(lo2f(vg.z), lo2f(vu.z)), act(hi2f(vg.z), hi2f(vu.z)))};
      }
      lds_barrier();
      f32x4 o0 = {0.f, 0.f, 0.f, 0.f}, o1 = {0.f, 0.f, 0.f, 0.f};
#pragma unroll
      for (int i = 0; i < 6; ++i) {
        const v4u a = *(const LAS v4u*)(L + L_IMG + (w + 8 * i) * 64 + kg * 16);
        if (i & 1) o1 = mfma(a, bd[i], o1); else o0 = mfma(a, bd[i], o0);
      }
      if (w < 6) o0 = mfma(*(const LAS v4u*)(L + L_IMG + (w + 48) * 64 + kg * 16), bd[6], o0);
      if (lane < 16) red[w * 128 + lane] = o0.x + o1.x;
      lds_barrier();
      if (w == 0 && lane < 16) {
        const float res = hres + rbf(sum8(red + lane));
        out[16 * td + lane] = (u16)pk2(res, res);
      }
      if (T == 63 && tid == 0) __hip_atomic_store((GAS unsigned*)(ws + WS_E), __builtin_amdgcn_readfirstlane(Ev) + 1u, __ATOMIC_RELAXED, __HIP_MEMORY_SCOPE_AGENT);
    }
  }
  asm volatile("s_waitcnt vmcnt(0)" :: "v"(pret) : "memory");
}

extern "C" void kernel_launch(void* const* d_in, const int* in_sizes, int n_in, void* d_out, int out_size, void* d_ws, size_t ws_size, hipStream_t stream) {
  (void)in_sizes; (void)n_in; (void)out_size; (void)ws_size;
  (void)hipFuncSetAttribute((const void*)fused_block, hipFuncAttributeMaxDynamicSharedMemorySize, (int)L_BYTES);
  hipLaunchKernelGGL(fused_block, dim3(NWG), dim3(NT), L_BYTES, stream, (const u16*)d_in[0], (const u16*)d_in[1], (const u16*)d_in[2], (const u16*)d_in[3],
                     (const u16*)d_in[4], (const u16*)d_in[6], (const u16*)d_in[7], (const u16*)d_in[8], (u16*)d_out, (unsigned char*)d_ws);
}
```
